```python
import torch

def _softmax(x, axis=-1):
    return torch.softmax(x, dim=axis)
def _gelu(x):
    return torch.nn.functional.gelu(x, approximate="tanh")
def _silu(x):
    return torch.nn.functional.silu(x)
def _sigmoid(x):
    return torch.sigmoid(x)
def _layernorm(x, axis=-1, eps=1e-5):
    m = x.mean(dim=axis, keepdim=True)
    v = x.var(dim=axis, keepdim=True, unbiased=False)
    return (x - m) / torch.sqrt(v + eps)
def _scatter_add(tgt, idx, src, axis=0):
    return tgt.clone().index_add_(axis, idx, src)
def _segment_sum(src, seg, n):
    out = torch.zeros((n,) + src.shape[1:], dtype=src.dtype)
    return out.index_add_(0, seg, src)
def _pad(x, pw):
    flat = tuple(v for pair in reversed(pw) for v in pair)
    return torch.nn.functional.pad(x, flat)

kernel_name = 'standalone__softmax_full_bidir__decode'

def setup_inputs():

    _w = (lambda a: (lambda t: t.to(torch.bfloat16) if t.is_floating_point() else t)(torch.as_tensor(a)))
    B, H, Sq, Skv, Dh = 1, 32, 1, 32768, 192
    return {
        "q": _w((lambda *s: torch.randn(*s, dtype=torch.bfloat16) * 0.8)(B, H, Sq, Dh)),
        "k": _w((lambda *s: torch.randn(*s, dtype=torch.bfloat16) * 0.8)(B, H, Skv, Dh)),
        "v": _w((lambda *s: torch.randn(*s, dtype=torch.bfloat16) * 0.05)(B, H, Skv, Dh)),
    }

def reference(q, k, v):
    scale = q.shape[-1] ** -0.5
    scores = torch.einsum('bhqd,bhkd->bhqk', q, k) * scale
    attn = _softmax(scores, axis=-1)
    return torch.einsum('bhqk,bhkd->bhqd', attn, v)
```

```cpp
#include <hip/hip_runtime.h>
#include <hip/hip_bf16.h>
#include <stdint.h>

namespace {

constexpr int H = 32, SKV = 32768, DH = 192;
constexpr int CPR = DH / 8;

typedef unsigned u32x4 __attribute__((ext_vector_type(4)));
typedef float f32x4 __attribute__((ext_vector_type(4)));
typedef __bf16 bf16x2_t __attribute__((ext_vector_type(2)));

__device__ __forceinline__ float dot2(unsigned a, unsigned b, float c) {
  return __builtin_amdgcn_fdot2_f32_bf16(__builtin_bit_cast(bf16x2_t, a), __builtin_bit_cast(bf16x2_t, b), c, false);
}
template <int CTRL>
__device__ __forceinline__ float dpp(float x) {
  return __builtin_bit_cast(float, __builtin_amdgcn_mov_dpp(__builtin_bit_cast(int, x), CTRL, 0xf, 0xf, true));
}
constexpr int XOR1 = 0xB1, XOR2 = 0x4E, HMIRROR = 0x141;

template <bool NT>
__device__ __forceinline__ u32x4 ldg(const u32x4* p) {
  if constexpr (NT) return __builtin_nontemporal_load(p);
  else return *p;
}

template <int NSPLIT, int WAVES, int U, int DEPTH, bool NT, bool ILV>
__global__ void __launch_bounds__(WAVES * 64)
attn_split(const u32x4* __restrict__ q, const u32x4* __restrict__ k, const u32x4* __restrict__ v, float* __restrict__ ws) {
  constexpr int KPS = SKV / NSPLIT;
  constexpr int UK = 8 * U;
  constexpr int NU = KPS / (UK * WAVES);
  static_assert(KPS % (UK * WAVES) == 0 && NU % DEPTH == 0 && NU >= DEPTH, "shape");
  __shared__ float lds[WAVES][DH + 4];

  const int head = blockIdx.x / NSPLIT, split = blockIdx.x % NSPLIT;
  const int tid = threadIdx.x, lane = tid & 63;
  const int wave = __builtin_amdgcn_readfirstlane(tid >> 6);
  const int r = lane >> 3, c = lane & 7;
  constexpr float C2 = 0.07216878364870322f * 1.4426950408889634f;

  const int key0 = split * KPS + (ILV ? wave * UK : wave * (KPS / WAVES));
  constexpr int USTRIDE = ILV ? UK * WAVES : UK;
  const size_t base = ((size_t)head * SKV + key0 + r) * CPR + c;
  const u32x4* kp = k + base;
  const u32x4* vp = v + base;

  u32x4 kb[DEPTH][U][3], vb[DEPTH][U][3];
#define ISSUE(slot, u)                                                                   \
  do {                                                                                   \
    const u32x4* kq_ = kp + (size_t)(u) * (USTRIDE * CPR);                               \
    const u32x4* vq_ = vp + (size_t)(u) * (USTRIDE * CPR);                               \
    _Pragma("unroll") for (int a_ = 0; a_ < U; ++a_) {                                   \
      _Pragma("unroll") for (int j_ = 0; j_ < 3; ++j_) kb[slot][a_][j_] = ldg<NT>(kq_ + a_ * 8 * CPR + j_ * 8); \
    }                                                                                    \
    _Pragma("unroll") for (int a_ = 0; a_ < U; ++a_) {                                   \
      _Pragma("unroll") for (int j_ = 0; j_ < 3; ++j_) vb[slot][a_][j_] = ldg<NT>(vq_ + a_ * 8 * CPR + j_ * 8); \
    }                                                                                    \
  } while (0)

  u32x4 qf[3];
#pragma unroll
  for (int j = 0; j < 3; ++j) qf[j] = q[head * CPR + j * 8 + c];
#pragma unroll
  for (int d = 0; d < DEPTH; ++d) ISSUE(d, d);

  float m = -INFINITY, l = 0.f;
  float acc[24];
#pragma unroll
  for (int i = 0; i < 24; ++i) acc[i] = 0.f;

  for (int u0 = 0; u0 < NU; u0 += DEPTH) {
#pragma unroll
    for (int d = 0; d < DEPTH; ++d) {
      float s[U];
#pragma unroll
      for (int a = 0; a < U; ++a) {
        float t0 = 0.f, t1 = 0.f, t2 = 0.f;
#pragma unroll
        for (int e = 0; e < 4; ++e) {
          t0 = dot2(kb[d][a][0][e], qf[0][e], t0);
          t1 = dot2(kb[d][a][1][e], qf[1][e], t1);
          t2 = dot2(kb[d][a][2][e], qf[2][e], t2);
        }
        float t = (t0 + t1) + t2;
        t += dpp<XOR1>(t);
        t += dpp<XOR2>(t);
        t += dpp<HMIRROR>(t);
        s[a] = t * C2;
      }
      float mn = m;
#pragma unroll
      for (int a = 0; a < U; ++a) mn = fmaxf(mn, s[a]);
      if (__any(mn > m)) {
        const float alpha = __builtin_amdgcn_exp2f(m - mn);
        l *= alpha;
#pragma unroll
        for (int i = 0; i < 24; ++i) acc[i] *= alpha;
        m = mn;
      }
#pragma unroll
      for (int a = 0; a < U; ++a) {
        const float p = __builtin_amdgcn_exp2f(s[a] - m);
        l += p;
#pragma unroll
        for (int j = 0; j < 3; ++j)
#pragma unroll
          for (int e = 0; e < 4; ++e) {
            const unsigned w = vb[d][a][j][e];
            acc[j * 8 + 2 * e] = fmaf(p, __builtin_bit_cast(float, w << 16), acc[j * 8 + 2 * e]);
            acc[j * 8 + 2 * e + 1] = fmaf(p, __builtin_bit_cast(float, w & 0xffff0000u), acc[j * 8 + 2 * e + 1]);
          }
      }
      if (u0 + d + DEPTH < NU) ISSUE(d, u0 + d + DEPTH);
    }
  }
#undef ISSUE

  float M = m;
  M = fmaxf(M, __shfl_xor(M, 8));
  M = fmaxf(M, __shfl_xor(M, 16));
  M = fmaxf(M, __shfl_xor(M, 32));
  const float f = __builtin_amdgcn_exp2f(m - M);
  l *= f;
  l += __shfl_xor(l, 8);
  l += __shfl_xor(l, 16);
  l += __shfl_xor(l, 32);
#pragma unroll
  for (int i = 0; i < 24; ++i) {
    float a = acc[i] * f;
    a += __shfl_xor(a, 8);
    a += __shfl_xor(a, 16);
    a += __shfl_xor(a, 32);
    acc[i] = a;
  }
  if (r == 0) {
#pragma unroll
    for (int j = 0; j < 3; ++j) {
      float* dst = &lds[wave][4 + (j * 8 + c) * 8];
      *reinterpret_cast<f32x4*>(dst) = f32x4{acc[j * 8], acc[j * 8 + 1], acc[j * 8 + 2], acc[j * 8 + 3]};
      *reinterpret_cast<f32x4*>(dst + 4) = f32x4{acc[j * 8 + 4], acc[j * 8 + 5], acc[j * 8 + 6], acc[j * 8 + 7]};
    }
    if (c == 0) { lds[wave][0] = M; lds[wave][1] = l; }
  }
  __syncthreads();
  if (tid < DH) {
    float MM = lds[0][0];
#pragma unroll
    for (int w = 1; w < WAVES; ++w) MM = fmaxf(MM, lds[w][0]);
    float L = 0.f, o = 0.f;
#pragma unroll
    for (int w = 0; w < WAVES; ++w) {
      const float wt = __builtin_amdgcn_exp2f(lds[w][0] - MM);
      L = fmaf(wt, lds[w][1], L);
      o = fmaf(wt, lds[w][4 + tid], o);
    }
    float* pml = ws;
    float* po = ws + H * NSPLIT * 2;
    const int slot = head * NSPLIT + split;
    po[(size_t)slot * DH + tid] = o;
    if (tid == 0) { pml[slot * 2] = MM; pml[slot * 2 + 1] = L; }
  }
}

template <int NSPLIT>
__global__ void __launch_bounds__(DH) attn_combine(const float* __restrict__ ws, __hip_bfloat16* __restrict__ out) {
  const int head = blockIdx.x, d = threadIdx.x;
  const float* pml = ws + head * NSPLIT * 2;
  const float* po = ws + H * NSPLIT * 2 + (size_t)head * NSPLIT * DH;
  float M = -INFINITY;
#pragma unroll
  for (int s = 0; s < NSPLIT; ++s) M = fmaxf(M, pml[2 * s]);
  float L = 0.f, o = 0.f;
#pragma unroll
  for (int s = 0; s < NSPLIT; ++s) {
    const float w = __builtin_amdgcn_exp2f(pml[2 * s] - M);
    L = fmaf(w, pml[2 * s + 1], L);
    o = fmaf(w, po[(size_t)s * DH + d], o);
  }
  out[head * DH + d] = __float2bfloat16(o / L);
}

}

#define CFG_NSPLIT 32
#define CFG_WAVES 4
#define CFG_U 2
#define CFG_DEPTH 2
#define CFG_NT true
#define CFG_ILV true

extern "C" void kernel_launch(void* const* d_in, const int* in_sizes, int n_in, void* d_out, int out_size, void* d_ws,
                              size_t ws_size, hipStream_t stream) {
  const u32x4* q = (const u32x4*)d_in[0];
  const u32x4* k = (const u32x4*)d_in[1];
  const u32x4* v = (const u32x4*)d_in[2];
  float* ws = (float*)d_ws;
  attn_split<CFG_NSPLIT, CFG_WAVES, CFG_U, CFG_DEPTH, CFG_NT, CFG_ILV>
      <<<H * CFG_NSPLIT, CFG_WAVES * 64, 0, stream>>>(q, k, v, ws);
  attn_combine<CFG_NSPLIT><<<H, DH, 0, stream>>>(ws, (__hip_bfloat16*)d_out);
}
```

```cpp
#include <hip/hip_runtime.h>
#include <hip/hip_bf16.h>
#include <stdint.h>

namespace {

constexpr int H = 32, SKV = 32768, DH = 192;
constexpr int CPR = DH / 8;

typedef unsigned u32x4 __attribute__((ext_vector_type(4)));
typedef float f32x4 __attribute__((ext_vector_type(4)));
typedef __bf16 bf16x2_t __attribute__((ext_vector_type(2)));

__device__ __forceinline__ float dot2(unsigned a, unsigned b, float c) {
  return __builtin_amdgcn_fdot2_f32_bf16(__builtin_bit_cast(bf16x2_t, a), __builtin_bit_cast(bf16x2_t, b), c, false);
}
template <int CTRL>
__device__ __forceinline__ float dpp(float x) {
  return __builtin_bit_cast(float, __builtin_amdgcn_mov_dpp(__builtin_bit_cast(int, x), CTRL, 0xf, 0xf, true));
}
constexpr int XOR1 = 0xB1, XOR2 = 0x4E, HMIRROR = 0x141;

template <bool NT>
__device__ __forceinline__ u32x4 ldg(const u32x4* p) {
  if constexpr (NT) return __builtin_nontemporal_load(p);
  else return *p;
}

template <int NSPLIT, int WAVES, int U, int DEPTH, bool NT, int ILV>
__global__ void __launch_bounds__(WAVES * 64)
attn_split(const u32x4* __restrict__ q, const u32x4* __restrict__ k, const u32x4* __restrict__ v, float* __restrict__ ws) {
  constexpr int KPS = SKV / NSPLIT;
  constexpr int UK = 8 * U;
  constexpr int NU = KPS / (UK * WAVES);
  static_assert(KPS % (UK * WAVES) == 0 && NU % DEPTH == 0 && NU >= DEPTH, "shape");
  __shared__ float lds[WAVES][DH + 4];

  const int head = blockIdx.x / NSPLIT, split = blockIdx.x % NSPLIT;
  const int tid = threadIdx.x, lane = tid & 63;
  const int wave = __builtin_amdgcn_readfirstlane(tid >> 6);
  const int r = lane >> 3, c = lane & 7;
  constexpr float C2 = 0.07216878364870322f * 1.4426950408889634f;

  const int key0 = ILV == 2 ? (split * WAVES + wave) * UK : split * KPS + (ILV ? wave * UK : wave * (KPS / WAVES));
  constexpr int USTRIDE = ILV == 2 ? UK * WAVES * NSPLIT : (ILV ? UK * WAVES : UK);
  const size_t base = ((size_t)head * SKV + key0 + r) * CPR + c;
  const u32x4* kp = k + base;
  const u32x4* vp = v + base;

  u32x4 kb[DEPTH][U][3], vb[DEPTH][U][3];
#define ISSUE(slot, u)                                                                   \
  do {                                                                                   \
    const u32x4* kq_ = kp + (size_t)(u) * (USTRIDE * CPR);                               \
    const u32x4* vq_ = vp + (size_t)(u) * (USTRIDE * CPR);                               \
    _Pragma("unroll") for (int a_ = 0; a_ < U; ++a_) {                                   \
      _Pragma("unroll") for (int j_ = 0; j_ < 3; ++j_) kb[slot][a_][j_] = ldg<NT>(kq_ + a_ * 8 * CPR + j_ * 8); \
    }                                                                                    \
    _Pragma("unroll") for (int a_ = 0; a_ < U; ++a_) {                                   \
      _Pragma("unroll") for (int j_ = 0; j_ < 3; ++j_) vb[slot][a_][j_] = ldg<NT>(vq_ + a_ * 8 * CPR + j_ * 8); \
    }                                                                                    \
  } while (0)

  u32x4 qf[3];
#pragma unroll
  for (int j = 0; j < 3; ++j) qf[j] = q[head * CPR + j * 8 + c];
#pragma unroll
  for (int d = 0; d < DEPTH; ++d) ISSUE(d, d);

  float m = -INFINITY, l = 0.f;
  float acc[24];
#pragma unroll
  for (int i = 0; i < 24; ++i) acc[i] = 0.f;

  for (int u0 = 0; u0 < NU; u0 += DEPTH) {
#pragma unroll
    for (int d = 0; d < DEPTH; ++d) {
      float s[U];
#pragma unroll
      for (int a = 0; a < U; ++a) {
        float t0 = 0.f, t1 = 0.f, t2 = 0.f;
#pragma unroll
        for (int e = 0; e < 4; ++e) {
          t0 = dot2(kb[d][a][0][e], qf[0][e], t0);
          t1 = dot2(kb[d][a][1][e], qf[1][e], t1);
          t2 = dot2(kb[d][a][2][e], qf[2][e], t2);
        }
        float t = (t0 + t1) + t2;
        t += dpp<XOR1>(t);
        t += dpp<XOR2>(t);
        t += dpp<HMIRROR>(t);
        s[a] = t * C2;
      }
      float mn = m;
#pragma unroll
      for (int a = 0; a < U; ++a) mn = fmaxf(mn, s[a]);
      if (__any(mn > m)) {
        const float alpha = __builtin_amdgcn_exp2f(m - mn);
        l *= alpha;
#pragma unroll
        for (int i = 0; i < 24; ++i) acc[i] *= alpha;
        m = mn;
      }
#pragma unroll
      for (int a = 0; a < U; ++a) {
        const float p = __builtin_amdgcn_exp2f(s[a] - m);
        l += p;
#pragma unroll
        for (int j = 0; j < 3; ++j)
#pragma unroll
          for (int e = 0; e < 4; ++e) {
            const unsigned w = vb[d][a][j][e];
            acc[j * 8 + 2 * e] = fmaf(p, __builtin_bit_cast(float, w << 16), acc[j * 8 + 2 * e]);
            acc[j * 8 + 2 * e + 1] = fmaf(p, __builtin_bit_cast(float, w & 0xffff0000u), acc[j * 8 + 2 * e + 1]);
          }
      }
      if (u0 + d + DEPTH < NU) ISSUE(d, u0 + d + DEPTH);
    }
  }
#undef ISSUE

  float M = m;
  M = fmaxf(M, __shfl_xor(M, 8));
  M = fmaxf(M, __shfl_xor(M, 16));
  M = fmaxf(M, __shfl_xor(M, 32));
  const float f = __builtin_amdgcn_exp2f(m - M);
  l *= f;
  l += __shfl_xor(l, 8);
  l += __shfl_xor(l, 16);
  l += __shfl_xor(l, 32);
#pragma unroll
  for (int i = 0; i < 24; ++i) {
    float a = acc[i] * f;
    a += __shfl_xor(a, 8);
    a += __shfl_xor(a, 16);
    a += __shfl_xor(a, 32);
    acc[i] = a;
  }
  if (r == 0) {
#pragma unroll
    for (int j = 0; j < 3; ++j) {
      float* dst = &lds[wave][4 + (j * 8 + c) * 8];
      *reinterpret_cast<f32x4*>(dst) = f32x4{acc[j * 8], acc[j * 8 + 1], acc[j * 8 + 2], acc[j * 8 + 3]};
      *reinterpret_cast<f32x4*>(dst + 4) = f32x4{acc[j * 8 + 4], acc[j * 8 + 5], acc[j * 8 + 6], acc[j * 8 + 7]};
    }
    if (c == 0) { lds[wave][0] = M; lds[wave][1] = l; }
  }
  __syncthreads();
  if (tid < DH) {
    float MM = lds[0][0];
#pragma unroll
    for (int w = 1; w < WAVES; ++w) MM = fmaxf(MM, lds[w][0]);
    float L = 0.f, o = 0.f;
#pragma unroll
    for (int w = 0; w < WAVES; ++w) {
      const float wt = __builtin_amdgcn_exp2f(lds[w][0] - MM);
      L = fmaf(wt, lds[w][1], L);
      o = fmaf(wt, lds[w][4 + tid], o);
    }
    float* pml = ws;
    float* po = ws + H * NSPLIT * 2;
    const int slot = head * NSPLIT + split;
    po[(size_t)slot * DH + tid] = o;
    if (tid == 0) { pml[slot * 2] = MM; pml[slot * 2 + 1] = L; }
  }
}

template <int NSPLIT>
__global__ void __launch_bounds__(DH) attn_combine(const float* __restrict__ ws, __hip_bfloat16* __restrict__ out) {
  const int head = blockIdx.x, d = threadIdx.x;
  const float* pml = ws + head * NSPLIT * 2;
  const float* po = ws + H * NSPLIT * 2 + (size_t)head * NSPLIT * DH;
  float M = -INFINITY;
#pragma unroll
  for (int s = 0; s < NSPLIT; ++s) M = fmaxf(M, pml[2 * s]);
  float L = 0.f, o = 0.f;
#pragma unroll
  for (int s = 0; s < NSPLIT; ++s) {
    const float w = __builtin_amdgcn_exp2f(pml[2 * s] - M);
    L = fmaf(w, pml[2 * s + 1], L);
    o = fmaf(w, po[(size_t)s * DH + d], o);
  }
  out[head * DH + d] = __float2bfloat16(o / L);
}

}

#define CFG_NSPLIT 32
#define CFG_WAVES 4
#define CFG_U 2
#define CFG_DEPTH 2
#define CFG_NT true
#define CFG_ILV 1

extern "C" void kernel_launch(void* const* d_in, const int* in_sizes, int n_in, void* d_out, int out_size, void* d_ws,
                              size_t ws_size, hipStream_t stream) {
  const u32x4* q = (const u32x4*)d_in[0];
  const u32x4* k = (const u32x4*)d_in[1];
  const u32x4* v = (const u32x4*)d_in[2];
  float* ws = (float*)d_ws;
  attn_split<CFG_NSPLIT, CFG_WAVES, CFG_U, CFG_DEPTH, CFG_NT, CFG_ILV>
      <<<H * CFG_NSPLIT, CFG_WAVES * 64, 0, stream>>>(q, k, v, ws);
  attn_combine<CFG_NSPLIT><<<H, DH, 0, stream>>>(ws, (__hip_bfloat16*)d_out);
}
```

```cpp
#include <hip/hip_runtime.h>
#include <hip/hip_bf16.h>
#include <stdint.h>

namespace {

constexpr int H = 32, SKV = 32768, DH = 192;
constexpr int CPR = DH / 8;

typedef unsigned u32x4 __attribute__((ext_vector_type(4)));
typedef float f32x4 __attribute__((ext_vector_type(4)));
typedef __bf16 bf16x2_t __attribute__((ext_vector_type(2)));

__device__ __forceinline__ float dot2(unsigned a, unsigned b, float c) {
  return __builtin_amdgcn_fdot2_f32_bf16(__builtin_bit_cast(bf16x2_t, a), __builtin_bit_cast(bf16x2_t, b), c, false);
}
template <int CTRL>
__device__ __forceinline__ float dpp(float x) {
  return __builtin_bit_cast(float, __builtin_amdgcn_mov_dpp(__builtin_bit_cast(int, x), CTRL, 0xf, 0xf, true));
}
constexpr int XOR1 = 0xB1, XOR2 = 0x4E, HMIRROR = 0x141;

template <int NSPLIT, int WAVES, int DEPTH>
__global__ void __launch_bounds__(WAVES * 64)
attn_split(const u32x4* __restrict__ q, const u32x4* __restrict__ k, const u32x4* __restrict__ v, float* __restrict__ ws) {
  constexpr int KPS = SKV / NSPLIT;
  constexpr int NU = KPS / (8 * WAVES);
  static_assert(KPS % (8 * WAVES) == 0 && NU % DEPTH == 0 && NU >= 2 * DEPTH && WAVES * 64 >= DH, "shape");
  __shared__ float lds[WAVES][DH + 4];

  const int head = blockIdx.x / NSPLIT, split = blockIdx.x % NSPLIT;
  const int tid = threadIdx.x, lane = tid & 63;
  const int wave = __builtin_amdgcn_readfirstlane(tid >> 6);
  const int r = lane >> 3, c = lane & 7;
  constexpr float C2 = 0.07216878364870322f * 1.4426950408889634f;
  constexpr int USTRIDE = 8 * WAVES;

  const size_t base = ((size_t)head * SKV + split * KPS + wave * 8 + r) * CPR + c;
  const u32x4* kp = k + base;
  const u32x4* vp = v + base;
  const int rot = (int)((blockIdx.x * 2654435761u >> 8) % (unsigned)NU);

  u32x4 kb[DEPTH][3], vb[DEPTH][3];
#define ISSUE(slot, u)                                                                                     \
  do {                                                                                                     \
    int ur_ = (u) + rot;                                                                                   \
    if (ur_ >= NU) ur_ -= NU;                                                                              \
    const u32x4* pk_ = kp + (size_t)ur_ * (USTRIDE * CPR);                                                 \
    const u32x4* pv_ = vp + (size_t)ur_ * (USTRIDE * CPR);                                                 \
    _Pragma("unroll") for (int j_ = 0; j_ < 3; ++j_) kb[slot][j_] = __builtin_nontemporal_load(pk_ + j_ * 8); \
    _Pragma("unroll") for (int j_ = 0; j_ < 3; ++j_) vb[slot][j_] = __builtin_nontemporal_load(pv_ + j_ * 8); \
  } while (0)

  u32x4 qf[3];
#pragma unroll
  for (int j = 0; j < 3; ++j) qf[j] = q[head * CPR + j * 8 + c];
#pragma unroll
  for (int d = 0; d < DEPTH; ++d) ISSUE(d, d);

  float m = -INFINITY, l = 0.f;
  float acc[24];
#pragma unroll
  for (int i = 0; i < 24; ++i) acc[i] = 0.f;

  auto compute = [&](const u32x4(&kk)[3], const u32x4(&vv)[3]) __attribute__((always_inline)) {
    float t0 = 0.f, t1 = 0.f, t2 = 0.f;
#pragma unroll
    for (int e = 0; e < 4; ++e) {
      t0 = dot2(kk[0][e], qf[0][e], t0);
      t1 = dot2(kk[1][e], qf[1][e], t1);
      t2 = dot2(kk[2][e], qf[2][e], t2);
    }
    float t = (t0 + t1) + t2;
    t += dpp<XOR1>(t);
    t += dpp<XOR2>(t);
    t += dpp<HMIRROR>(t);
    const float s = t * C2;
    const float mn = fmaxf(m, s);
    if (__any(mn > m)) {
      const float alpha = __builtin_amdgcn_exp2f(m - mn);
      l *= alpha;
#pragma unroll
      for (int i = 0; i < 24; ++i) acc[i] *= alpha;
      m = mn;
    }
    const float p = __builtin_amdgcn_exp2f(s - m);
    l += p;
#pragma unroll
    for (int j = 0; j < 3; ++j)
#pragma unroll
      for (int e = 0; e < 4; ++e) {
        const unsigned w = vv[j][e];
        acc[j * 8 + 2 * e] = fmaf(p, __builtin_bit_cast(float, w << 16), acc[j * 8 + 2 * e]);
        acc[j * 8 + 2 * e + 1] = fmaf(p, __builtin_bit_cast(float, w & 0xffff0000u), acc[j * 8 + 2 * e + 1]);
      }
  };

#pragma unroll 1
  for (int u0 = 0; u0 < NU - DEPTH; u0 += DEPTH) {
#pragma unroll
    for (int d = 0; d < DEPTH; ++d) {
      compute(kb[d], vb[d]);
      ISSUE(d, u0 + d + DEPTH);
    }
  }
#pragma unroll
  for (int d = 0; d < DEPTH; ++d) compute(kb[d], vb[d]);
#undef ISSUE

  float M = m;
  M = fmaxf(M, __shfl_xor(M, 8));
  M = fmaxf(M, __shfl_xor(M, 16));
  M = fmaxf(M, __shfl_xor(M, 32));
  const float f = __builtin_amdgcn_exp2f(m - M);
  l *= f;
  l += __shfl_xor(l, 8);
  l += __shfl_xor(l, 16);
  l += __shfl_xor(l, 32);
#pragma unroll
  for (int i = 0; i < 24; ++i) {
    float a = acc[i] * f;
    a += __shfl_xor(a, 8);
    a += __shfl_xor(a, 16);
    a += __shfl_xor(a, 32);
    acc[i] = a;
  }
  if (r == 0) {
#pragma unroll
    for (int j = 0; j < 3; ++j) {
      float* dst = &lds[wave][4 + (j * 8 + c) * 8];
      *reinterpret_cast<f32x4*>(dst) = f32x4{acc[j * 8], acc[j * 8 + 1], acc[j * 8 + 2], acc[j * 8 + 3]};
      *reinterpret_cast<f32x4*>(dst + 4) = f32x4{acc[j * 8 + 4], acc[j * 8 + 5], acc[j * 8 + 6], acc[j * 8 + 7]};
    }
    if (c == 0) {
      lds[wave][0] = M;
      lds[wave][1] = l;
    }
  }
  __syncthreads();
  if (tid < DH) {
    float MM = lds[0][0];
#pragma unroll
    for (int w = 1; w < WAVES; ++w) MM = fmaxf(MM, lds[w][0]);
    float L = 0.f, o = 0.f;
#pragma unroll
    for (int w = 0; w < WAVES; ++w) {
      const float wt = __builtin_amdgcn_exp2f(lds[w][0] - MM);
      L = fmaf(wt, lds[w][1], L);
      o = fmaf(wt, lds[w][4 + tid], o);
    }
    float* pml = ws;
    float* po = ws + H * NSPLIT * 2;
    const int slot = head * NSPLIT + split;
    po[(size_t)slot * DH + tid] = o;
    if (tid == 0) {
      pml[slot * 2] = MM;
      pml[slot * 2 + 1] = L;
    }
  }
}

template <int NSPLIT>
__global__ void __launch_bounds__(DH) attn_combine(const float* __restrict__ ws, __hip_bfloat16* __restrict__ out) {
  const int head = blockIdx.x, d = threadIdx.x;
  const float* pml = ws + head * NSPLIT * 2;
  const float* po = ws + H * NSPLIT * 2 + (size_t)head * NSPLIT * DH;
  float M = -INFINITY;
#pragma unroll
  for (int s = 0; s < NSPLIT; ++s) M = fmaxf(M, pml[2 * s]);
  float L = 0.f, o = 0.f;
#pragma unroll
  for (int s = 0; s < NSPLIT; ++s) {
    const float w = __builtin_amdgcn_exp2f(pml[2 * s] - M);
    L = fmaf(w, pml[2 * s + 1], L);
    o = fmaf(w, po[(size_t)s * DH + d], o);
  }
  out[head * DH + d] = __float2bfloat16(o / L);
}

constexpr int NSPLIT = 16, WAVES = 4, DEPTH = 4;

}

extern "C" void kernel_launch(void* const* d_in, const int* in_sizes, int n_in, void* d_out, int out_size, void* d_ws,
                              size_t ws_size, hipStream_t stream) {
  const u32x4* q = (const u32x4*)d_in[0];
  const u32x4* k = (const u32x4*)d_in[1];
  const u32x4* v = (const u32x4*)d_in[2];
  float* ws = (float*)d_ws;
  attn_split<NSPLIT, WAVES, DEPTH><<<H * NSPLIT, WAVES * 64, 0, stream>>>(q, k, v, ws);
  attn_combine<NSPLIT><<<H, DH, 0, stream>>>(ws, (__hip_bfloat16*)d_out);
}
```
